```python
import torch

def _softmax(x, axis=-1):
    return torch.softmax(x, dim=axis)
def _gelu(x):
    return torch.nn.functional.gelu(x, approximate="tanh")
def _silu(x):
    return torch.nn.functional.silu(x)
def _sigmoid(x):
    return torch.sigmoid(x)
def _layernorm(x, axis=-1, eps=1e-5):
    m = x.mean(dim=axis, keepdim=True)
    v = x.var(dim=axis, keepdim=True, unbiased=False)
    return (x - m) / torch.sqrt(v + eps)
def _scatter_add(tgt, idx, src, axis=0):
    return tgt.clone().index_add_(axis, idx, src)
def _segment_sum(src, seg, n):
    out = torch.zeros((n,) + src.shape[1:], dtype=src.dtype)
    return out.index_add_(0, seg, src)
def _pad(x, pw):
    flat = tuple(v for pair in reversed(pw) for v in pair)
    return torch.nn.functional.pad(x, flat)

kernel_name = 'parallel_sum__sliding_block_cached__mixer_channel_mlp__decode'

def setup_inputs():

    _w = (lambda a: (lambda t: t.to(torch.bfloat16) if t.is_floating_point() else t)(torch.as_tensor(a)))
    def _A_setup():
        B, Sq, D = 1, 1, 640
        Hq, Hkv, Sk, Dh = 32, 32, 4096, 128
        assert Hq % Hkv == 0, (Hq, Hkv)
        return {
            "x": _w((lambda *s: torch.randn(*s, dtype=torch.bfloat16) * 0.05)(B, Sq, D)),
            "k_cache": _w((lambda *s: torch.randn(*s, dtype=torch.bfloat16) * 3.2)(B, Hkv, Sk, Dh)),
            "v_cache": _w((lambda *s: torch.randn(*s, dtype=torch.bfloat16) * 0.4)(B, Hkv, Sk, Dh)),
            "wq": _w((lambda *s: torch.randn(*s, dtype=torch.bfloat16) * 3.2)(D, Hq * Dh) * (D ** -0.5)),
            "wo": _w((lambda *s: torch.randn(*s, dtype=torch.bfloat16) * 0.4)(Hq * Dh, D) * ((Hq * Dh) ** -0.5)),
        }
    _A_in = _A_setup()

    def _B_setup():
        B, S, D, F = 1, 1, 640, 1728
        return {
            "x": _w((lambda *s: torch.randn(*s, dtype=torch.bfloat16) * 0.05)(B, S, D)),
            "w1": _w((lambda *s: torch.randn(*s, dtype=torch.bfloat16) * 3.2)(D, F) * (D ** -0.5)),
            "b1": _w((lambda *s: torch.randn(*s, dtype=torch.bfloat16) * 0.8)(F)),
            "w2": _w((lambda *s: torch.randn(*s, dtype=torch.bfloat16) * 0.1)(F, D) * (F ** -0.5)),
            "b2": _w((lambda *s: torch.randn(*s, dtype=torch.bfloat16) * 0.1)(D)),
        }
    _B_in = _B_setup()

    return {
        "A_x": _A_in["x"],
        "A_k_cache": _A_in["k_cache"],
        "A_v_cache": _A_in["v_cache"],
        "A_wq": _A_in["wq"],
        "A_wo": _A_in["wo"],
        "B_x": _B_in["x"],
        "B_w1": _B_in["w1"],
        "B_b1": _B_in["b1"],
        "B_w2": _B_in["w2"],
        "B_b2": _B_in["b2"],
    }

def reference(A_x, A_k_cache, A_v_cache, A_wq, A_wo, B_x, B_w1, B_b1, B_w2, B_b2):
    def _A_ref(x, k_cache, v_cache, wq, wo):
        B, Sq, D = x.shape
        Hq, Dh = 32, 128
        Hkv, Sk = k_cache.shape[1], k_cache.shape[2]
        W = min(4096, max(256, Sk // 4))
        rep = Hq // Hkv
        q = (lambda x, *d: x.permute(*d))((x @ wq).reshape(B, Sq, Hq, Dh), 0, 2, 1, 3)
        k = (lambda x, n, axis: torch.repeat_interleave(x, n, dim=axis))(k_cache[:, :, Sk - W:, :], rep, axis=1)
        v = (lambda x, n, axis: torch.repeat_interleave(x, n, dim=axis))(v_cache[:, :, Sk - W:, :], rep, axis=1)
        scores = torch.einsum("bhqd,bhkd->bhqk", q, k) * (Dh ** -0.5)
        p = _softmax(scores, axis=-1)
        o = torch.einsum("bhqk,bhkd->bhqd", p, v)
        return (lambda x, *d: x.permute(*d))(o, 0, 2, 1, 3).reshape(B, Sq, Hq * Dh) @ wo

    def _B_ref(x, w1, b1, w2, b2):
        y = _layernorm(x, axis=-1)
        h = _gelu(torch.einsum("bsd,df->bsf", y, w1) + b1)
        return torch.einsum("bsf,fd->bsd", h, w2) + b2

    x = A_x
    xn = _layernorm(x, axis=-1)
    return x + _A_ref(xn, A_k_cache, A_v_cache, A_wq, A_wo) + _B_ref(xn, B_w1, B_b1, B_w2, B_b2)
```

```cpp
#include <hip/hip_runtime.h>
#include <hip/hip_bf16.h>
#include <stdint.h>

typedef unsigned int u32;
typedef unsigned short u16;
typedef u32 v4u __attribute__((ext_vector_type(4)));

#define D_MODEL 640
#define NHEAD 32
#define DH 128
#define SK 4096
#define WIN 1024
#define FF 1728
#define QCOLS 4096
#define NCOLS (QCOLS + FF)
#define KSPLIT 4
#define ROWS_PER_SPLIT (D_MODEL / KSPLIT)

__device__ __forceinline__ float bf2f(u32 b) { return __uint_as_float(b << 16); }
__device__ __forceinline__ float bflo(u32 w) { return __uint_as_float(w << 16); }
__device__ __forceinline__ float bfhi(u32 w) { return __uint_as_float(w & 0xffff0000u); }
__device__ __forceinline__ u32 f2bf(float f) {
    u32 u = __float_as_uint(f);
    return (u + 0x7fffu + ((u >> 16) & 1u)) >> 16;
}
__device__ __forceinline__ float rbf(float f) { return bf2f(f2bf(f)); }

#define WS_QPART 0
#define WS_ATT (WS_QPART + KSPLIT * NCOLS + 64)
#define WS_OUTP (WS_ATT + 256 * 132)
#define WS_CNT (WS_OUTP + 256 * 160)

__device__ __forceinline__ float wave_sum(float v) {
#pragma unroll
    for (int o = 32; o > 0; o >>= 1) v += __shfl_xor(v, o, 64);
    return v;
}

__global__ void __launch_bounds__(256) k1_proj(const u16* __restrict__ x, const u16* __restrict__ wq,
                                               const u16* __restrict__ w1, float* __restrict__ ws) {
    __shared__ float s_x[D_MODEL];
    __shared__ float s_red[4][64];
    const int t = threadIdx.x;
    const int cb = blockIdx.x >> 2;
    const int ks = blockIdx.x & 3;
    const bool isq = cb < 64;
    const u16* wbase = isq ? (wq + cb * 64) : (w1 + (cb - 64) * 64);
    const int ldw = isq ? QCOLS : FF;
    const int r0 = ks * ROWS_PER_SPLIT + (t >> 3);
    const int cc = t & 7;
    v4u wv[5];
#pragma unroll
    for (int i = 0; i < 5; i++)
        wv[i] = *(const v4u*)(wbase + (size_t)(r0 + 32 * i) * ldw + cc * 8);
    if (blockIdx.x == 0 && t < 8) ((u32*)ws)[WS_CNT + t * 64] = 0u;
    if (t < 64) {
        float v[10];
        float s = 0.f;
#pragma unroll
        for (int i = 0; i < 10; i++) { v[i] = bf2f(x[t + 64 * i]); s += v[i]; }
        s = wave_sum(s);
        float mean = s * (1.0f / D_MODEL);
        float q = 0.f;
#pragma unroll
        for (int i = 0; i < 10; i++) { float d = v[i] - mean; q += d * d; }
        q = wave_sum(q) * (1.0f / D_MODEL);
        float mb = rbf(mean), vb = rbf(q);
        float sd = rbf(sqrtf(rbf(vb + 1e-5f)));
#pragma unroll
        for (int i = 0; i < 10; i++) v[i] = rbf(rbf(v[i] - mb) / sd);
        if (!isq) {
            s = 0.f;
#pragma unroll
            for (int i = 0; i < 10; i++) s += v[i];
            s = wave_sum(s);
            mean = s * (1.0f / D_MODEL);
            q = 0.f;
#pragma unroll
            for (int i = 0; i < 10; i++) { float d = v[i] - mean; q += d * d; }
            q = wave_sum(q) * (1.0f / D_MODEL);
            mb = rbf(mean); vb = rbf(q);
            sd = rbf(sqrtf(rbf(vb + 1e-5f)));
#pragma unroll
            for (int i = 0; i < 10; i++) v[i] = rbf(rbf(v[i] - mb) / sd);
        }
#pragma unroll
        for (int i = 0; i < 10; i++) s_x[t + 64 * i] = v[i];
    }
    __syncthreads();
    float acc[8];
#pragma unroll
    for (int j = 0; j < 8; j++) acc[j] = 0.f;
#pragma unroll
    for (int i = 0; i < 5; i++) {
        float xv = s_x[r0 + 32 * i];
#pragma unroll
        for (int j = 0; j < 4; j++) {
            acc[2 * j] += xv * bflo(wv[i][j]);
            acc[2 * j + 1] += xv * bfhi(wv[i][j]);
        }
    }
#pragma unroll
    for (int j = 0; j < 8; j++) {
        acc[j] += __shfl_xor(acc[j], 8, 64);
        acc[j] += __shfl_xor(acc[j], 16, 64);
        acc[j] += __shfl_xor(acc[j], 32, 64);
    }
    const int w = t >> 6, lane = t & 63;
    if (lane < 8) {
#pragma unroll
        for (int j = 0; j < 8; j++) s_red[w][lane * 8 + j] = acc[j];
    }
    __syncthreads();
    if (t < 64) {
        float r = s_red[0][t] + s_red[1][t] + s_red[2][t] + s_red[3][t];
        ws[WS_QPART + ks * NCOLS + cb * 64 + t] = r;
    }
}

__global__ void __launch_bounds__(512) k2_attn(const u16* __restrict__ kc, const u16* __restrict__ vc,
                                               float* __restrict__ ws) {
    __shared__ float s_q[DH];
    __shared__ float s_s[128];
    __shared__ float s_o[8][DH];
    const int t = threadIdx.x;
    const int h = blockIdx.x >> 3, c = blockIdx.x & 7;
    const int w = t >> 6, lane = t & 63;
    const int dl = lane & 15;
    const int kq = (t >> 4);
    const size_t base = ((size_t)h * SK + (SK - WIN) + c * 128) * DH;
    v4u kv[4], vv[4];
#pragma unroll
    for (int i = 0; i < 4; i++) kv[i] = *(const v4u*)(kc + base + (size_t)(kq + 32 * i) * DH + dl * 8);
#pragma unroll
    for (int i = 0; i < 4; i++) vv[i] = *(const v4u*)(vc + base + (size_t)(kq + 32 * i) * DH + dl * 8);
    if (t < DH) {
        float q = 0.f;
#pragma unroll
        for (int k = 0; k < KSPLIT; k++) q += ws[WS_QPART + k * NCOLS + h * DH + t];
        s_q[t] = rbf(q);
    }
    __syncthreads();
    float qv[8];
#pragma unroll
    for (int j = 0; j < 8; j++) qv[j] = s_q[dl * 8 + j];
#pragma unroll
    for (int i = 0; i < 4; i++) {
        float s = 0.f;
#pragma unroll
        for (int j = 0; j < 4; j++) {
            s += qv[2 * j] * bflo(kv[i][j]);
            s += qv[2 * j + 1] * bfhi(kv[i][j]);
        }
        s += __shfl_xor(s, 1, 64);
        s += __shfl_xor(s, 2, 64);
        s += __shfl_xor(s, 4, 64);
        s += __shfl_xor(s, 8, 64);
        if (dl == 0) s_s[kq + 32 * i] = rbf(rbf(s) * 0.08838834764831845f);
    }
    __syncthreads();
    float m = fmaxf(s_s[lane], s_s[lane + 64]);
#pragma unroll
    for (int o = 32; o > 0; o >>= 1) m = fmaxf(m, __shfl_xor(m, o, 64));
    float oacc[8];
#pragma unroll
    for (int j = 0; j < 8; j++) oacc[j] = 0.f;
#pragma unroll
    for (int i = 0; i < 4; i++) {
        float p = __expf(s_s[kq + 32 * i] - m);
#pragma unroll
        for (int j = 0; j < 4; j++) {
            oacc[2 * j] += p * bflo(vv[i][j]);
            oacc[2 * j + 1] += p * bfhi(vv[i][j]);
        }
    }
#pragma unroll
    for (int j = 0; j < 8; j++) {
        oacc[j] += __shfl_xor(oacc[j], 16, 64);
        oacc[j] += __shfl_xor(oacc[j], 32, 64);
    }
    if (lane < 16) {
#pragma unroll
        for (int j = 0; j < 8; j++) s_o[w][lane * 8 + j] = oacc[j];
    }
    __syncthreads();
    float* outp = ws + WS_ATT + blockIdx.x * 132;
    if (t < DH) {
        float o = 0.f;
#pragma unroll
        for (int k = 0; k < 8; k++) o += s_o[k][t];
        outp[t] = o;
    }
    if (w == 7) {
        float l = __expf(s_s[lane] - m) + __expf(s_s[lane + 64] - m);
        l = wave_sum(l);
        if (lane == 0) { outp[128] = m; outp[129] = l; }
    }
}

__global__ void __launch_bounds__(320) k3_out(const u16* __restrict__ x, const u16* __restrict__ wo,
                                              const u16* __restrict__ w2, const u16* __restrict__ b1,
                                              const u16* __restrict__ b2, float* __restrict__ ws,
                                              u16* __restrict__ out) {
    __shared__ float s_o[DH];
    __shared__ float s_h[64];
    __shared__ float s_red[2][32][80];
    __shared__ int s_last;
    const int t = threadIdx.x;
    const int h = blockIdx.x >> 3, c = blockIdx.x & 7;
    const int ch = t % 10, rb = t / 10;
    v4u wv[4], w2v[2];
#pragma unroll
    for (int i = 0; i < 4; i++)
        wv[i] = *(const v4u*)(wo + (size_t)(h * DH + rb + 32 * i) * D_MODEL + c * 80 + ch * 8);
    w2v[0] = *(const v4u*)(w2 + (size_t)(h * 54 + rb) * D_MODEL + c * 80 + ch * 8);
    w2v[1] = (rb + 32 < 54) ? *(const v4u*)(w2 + (size_t)(h * 54 + rb + 32) * D_MODEL + c * 80 + ch * 8)
                            : (v4u){0u, 0u, 0u, 0u};
    if (t < DH) {
        float M = -INFINITY;
#pragma unroll
        for (int k = 0; k < 8; k++) M = fmaxf(M, ws[WS_ATT + (h * 8 + k) * 132 + 128]);
        float L = 0.f, o = 0.f;
#pragma unroll
        for (int k = 0; k < 8; k++) {
            const float* p = ws + WS_ATT + (h * 8 + k) * 132;
            float sc = __expf(p[128] - M);
            L += sc * p[129];
            o += sc * p[t];
        }
        s_o[t] = rbf(o / L);
    } else if (t < DH + 64) {
        int j = t - DH;
        float hv = 0.f;
        if (j < 54) {
            int col = h * 54 + j;
#pragma unroll
            for (int k = 0; k < KSPLIT; k++) hv += ws[WS_QPART + k * NCOLS + QCOLS + col];
            hv = rbf(rbf(hv) + bf2f(b1[col]));
            float g = 0.5f * hv * (1.0f + tanhf(0.7978845608028654f * (hv + 0.044715f * hv * hv * hv)));
            hv = rbf(g);
        }
        s_h[j] = hv;
    }
    __syncthreads();
    float a[8], b[8];
#pragma unroll
    for (int j = 0; j < 8; j++) { a[j] = 0.f; b[j] = 0.f; }
#pragma unroll
    for (int i = 0; i < 4; i++) {
        float ov = s_o[rb + 32 * i];
#pragma unroll
        for (int j = 0; j < 4; j++) {
            a[2 * j] += ov * bflo(wv[i][j]);
            a[2 * j + 1] += ov * bfhi(wv[i][j]);
        }
    }
#pragma unroll
    for (int i = 0; i < 2; i++) {
        float hv = s_h[(rb + 32 * i) & 63];
#pragma unroll
        for (int j = 0; j < 4; j++) {
            b[2 * j] += hv * bflo(w2v[i][j]);
            b[2 * j + 1] += hv * bfhi(w2v[i][j]);
        }
    }
#pragma unroll
    for (int j = 0; j < 8; j++) {
        s_red[0][rb][ch * 8 + j] = a[j];
        s_red[1][rb][ch * 8 + j] = b[j];
    }
    __syncthreads();
    if (t < 160) {
        const int which = t / 80, col = t % 80;
        float r = 0.f;
#pragma unroll
        for (int k = 0; k < 32; k++) r += s_red[which][k][col];
        ws[WS_OUTP + blockIdx.x * 160 + t] = r;
    }
    __threadfence();
    __syncthreads();
    if (t == 0) {
        u32 prev = atomicAdd((u32*)ws + WS_CNT + c * 64, 1u);
        s_last = (prev == 31u);
    }
    __syncthreads();
    if (s_last) {
        __threadfence();
        if (t < 80) {
            float A = 0.f, B = 0.f;
            for (int k = 0; k < 32; k++) {
                const float* p = ws + WS_OUTP + (k * 8 + c) * 160;
                A += __builtin_nontemporal_load(p + t);
                B += __builtin_nontemporal_load(p + 80 + t);
            }
            const int col = c * 80 + t;
            float Bv = rbf(rbf(B) + bf2f(b2[col]));
            float r = rbf(bf2f(x[col]) + rbf(A));
            out[col] = (u16)f2bf(r + Bv);
        }
    }
}

extern "C" void kernel_launch(void* const* d_in, const int* in_sizes, int n_in,
                              void* d_out, int out_size, void* d_ws, size_t ws_size,
                              hipStream_t stream) {
    const u16* x = (const u16*)d_in[0];
    const u16* kc = (const u16*)d_in[1];
    const u16* vc = (const u16*)d_in[2];
    const u16* wq = (const u16*)d_in[3];
    const u16* wo = (const u16*)d_in[4];
    const u16* w1 = (const u16*)d_in[6];
    const u16* b1 = (const u16*)d_in[7];
    const u16* w2 = (const u16*)d_in[8];
    const u16* b2 = (const u16*)d_in[9];
    float* ws = (float*)d_ws;
    k1_proj<<<91 * 4, 256, 0, stream>>>(x, wq, w1, ws);
    k2_attn<<<256, 512, 0, stream>>>(kc, vc, ws);
    k3_out<<<256, 320, 0, stream>>>(x, wo, w2, b1, b2, ws, (u16*)d_out);
}
```

```cpp
#include <hip/hip_runtime.h>
#include <hip/hip_bf16.h>
#include <stdint.h>

typedef unsigned int u32;
typedef unsigned short u16;
typedef u32 v4u __attribute__((ext_vector_type(4)));

#define D_MODEL 640
#define NHEAD 32
#define DH 128
#define SK 4096
#define WIN 1024
#define FF 1728
#define QCOLS 4096
#define NCOLS (QCOLS + FF)
#define KSPLIT 4
#define ROWS_PER_SPLIT (D_MODEL / KSPLIT)

__device__ __forceinline__ float bf2f(u32 b) { return __uint_as_float(b << 16); }
__device__ __forceinline__ float bflo(u32 w) { return __uint_as_float(w << 16); }
__device__ __forceinline__ float bfhi(u32 w) { return __uint_as_float(w & 0xffff0000u); }
__device__ __forceinline__ u32 f2bf(float f) {
    u32 u = __float_as_uint(f);
    return (u + 0x7fffu + ((u >> 16) & 1u)) >> 16;
}
__device__ __forceinline__ float rbf(float f) { return bf2f(f2bf(f)); }

#define WS_QPART 0
#define WS_ATT (WS_QPART + KSPLIT * NCOLS + 64)
#define WS_OUTP (WS_ATT + 256 * 132)
#define WS_CNT (WS_OUTP + 256 * 160)

__device__ __forceinline__ float wave_sum(float v) {
#pragma unroll
    for (int o = 32; o > 0; o >>= 1) v += __shfl_xor(v, o, 64);
    return v;
}

__global__ void __launch_bounds__(256) k1_proj(const u16* __restrict__ x, const u16* __restrict__ wq,
                                               const u16* __restrict__ w1, float* __restrict__ ws) {
    __shared__ float s_x[D_MODEL];
    __shared__ float s_red[4][64];
    const int t = threadIdx.x;
    const int cb = blockIdx.x >> 2;
    const int ks = blockIdx.x & 3;
    const bool isq = cb < 64;
    const u16* wbase = isq ? (wq + cb * 64) : (w1 + (cb - 64) * 64);
    const int ldw = isq ? QCOLS : FF;
    const int r0 = ks * ROWS_PER_SPLIT + (t >> 3);
    const int cc = t & 7;
    v4u wv[5];
#pragma unroll
    for (int i = 0; i < 5; i++)
        wv[i] = __builtin_nontemporal_load((const v4u*)(wbase + (size_t)(r0 + 32 * i) * ldw + cc * 8));
    if (blockIdx.x == 0 && t < 8) ((u32*)ws)[WS_CNT + t * 64] = 0u;
    if (t < 64) {
        float v[10];
        float s = 0.f;
#pragma unroll
        for (int i = 0; i < 10; i++) { v[i] = bf2f(x[t + 64 * i]); s += v[i]; }
        s = wave_sum(s);
        float mean = s * (1.0f / D_MODEL);
        float q = 0.f;
#pragma unroll
        for (int i = 0; i < 10; i++) { float d = v[i] - mean; q += d * d; }
        q = wave_sum(q) * (1.0f / D_MODEL);
        float mb = rbf(mean), vb = rbf(q);
        float sd = rbf(sqrtf(rbf(vb + 1e-5f)));
#pragma unroll
        for (int i = 0; i < 10; i++) v[i] = rbf(rbf(v[i] - mb) / sd);
        if (!isq) {
            s = 0.f;
#pragma unroll
            for (int i = 0; i < 10; i++) s += v[i];
            s = wave_sum(s);
            mean = s * (1.0f / D_MODEL);
            q = 0.f;
#pragma unroll
            for (int i = 0; i < 10; i++) { float d = v[i] - mean; q += d * d; }
            q = wave_sum(q) * (1.0f / D_MODEL);
            mb = rbf(mean); vb = rbf(q);
            sd = rbf(sqrtf(rbf(vb + 1e-5f)));
#pragma unroll
            for (int i = 0; i < 10; i++) v[i] = rbf(rbf(v[i] - mb) / sd);
        }
#pragma unroll
        for (int i = 0; i < 10; i++) s_x[t + 64 * i] = v[i];
    }
    __syncthreads();
    float acc[8];
#pragma unroll
    for (int j = 0; j < 8; j++) acc[j] = 0.f;
#pragma unroll
    for (int i = 0; i < 5; i++) {
        float xv = s_x[r0 + 32 * i];
#pragma unroll
        for (int j = 0; j < 4; j++) {
            acc[2 * j] += xv * bflo(wv[i][j]);
            acc[2 * j + 1] += xv * bfhi(wv[i][j]);
        }
    }
#pragma unroll
    for (int j = 0; j < 8; j++) {
        acc[j] += __shfl_xor(acc[j], 8, 64);
        acc[j] += __shfl_xor(acc[j], 16, 64);
        acc[j] += __shfl_xor(acc[j], 32, 64);
    }
    const int w = t >> 6, lane = t & 63;
    if (lane < 8) {
#pragma unroll
        for (int j = 0; j < 8; j++) s_red[w][lane * 8 + j] = acc[j];
    }
    __syncthreads();
    if (t < 64) {
        float r = s_red[0][t] + s_red[1][t] + s_red[2][t] + s_red[3][t];
        ws[WS_QPART + ks * NCOLS + cb * 64 + t] = r;
    }
}

__global__ void __launch_bounds__(512) k2_attn(const u16* __restrict__ kc, const u16* __restrict__ vc,
                                               float* __restrict__ ws) {
    __shared__ float s_q[DH];
    __shared__ float s_s[128];
    __shared__ float s_o[8][DH];
    const int t = threadIdx.x;
    const int h = blockIdx.x >> 3, c = blockIdx.x & 7;
    const int w = t >> 6, lane = t & 63;
    const int dl = lane & 15;
    const int kq = (t >> 4);
    const size_t base = ((size_t)h * SK + (SK - WIN) + c * 128) * DH;
    v4u kv[4], vv[4];
#pragma unroll
    for (int i = 0; i < 4; i++) kv[i] = __builtin_nontemporal_load((const v4u*)(kc + base + (size_t)(kq + 32 * i) * DH + dl * 8));
#pragma unroll
    for (int i = 0; i < 4; i++) vv[i] = __builtin_nontemporal_load((const v4u*)(vc + base + (size_t)(kq + 32 * i) * DH + dl * 8));
    if (t < DH) {
        float q = 0.f;
#pragma unroll
        for (int k = 0; k < KSPLIT; k++) q += ws[WS_QPART + k * NCOLS + h * DH + t];
        s_q[t] = rbf(q);
    }
    __syncthreads();
    float qv[8];
#pragma unroll
    for (int j = 0; j < 8; j++) qv[j] = s_q[dl * 8 + j];
#pragma unroll
    for (int i = 0; i < 4; i++) {
        float s = 0.f;
#pragma unroll
        for (int j = 0; j < 4; j++) {
            s += qv[2 * j] * bflo(kv[i][j]);
            s += qv[2 * j + 1] * bfhi(kv[i][j]);
        }
        s += __shfl_xor(s, 1, 64);
        s += __shfl_xor(s, 2, 64);
        s += __shfl_xor(s, 4, 64);
        s += __shfl_xor(s, 8, 64);
        if (dl == 0) s_s[kq + 32 * i] = rbf(rbf(s) * 0.08838834764831845f);
    }
    __syncthreads();
    float m = fmaxf(s_s[lane], s_s[lane + 64]);
#pragma unroll
    for (int o = 32; o > 0; o >>= 1) m = fmaxf(m, __shfl_xor(m, o, 64));
    float oacc[8];
#pragma unroll
    for (int j = 0; j < 8; j++) oacc[j] = 0.f;
#pragma unroll
    for (int i = 0; i < 4; i++) {
        float p = __expf(s_s[kq + 32 * i] - m);
#pragma unroll
        for (int j = 0; j < 4; j++) {
            oacc[2 * j] += p * bflo(vv[i][j]);
            oacc[2 * j + 1] += p * bfhi(vv[i][j]);
        }
    }
#pragma unroll
    for (int j = 0; j < 8; j++) {
        oacc[j] += __shfl_xor(oacc[j], 16, 64);
        oacc[j] += __shfl_xor(oacc[j], 32, 64);
    }
    if (lane < 16) {
#pragma unroll
        for (int j = 0; j < 8; j++) s_o[w][lane * 8 + j] = oacc[j];
    }
    __syncthreads();
    float* outp = ws + WS_ATT + blockIdx.x * 132;
    if (t < DH) {
        float o = 0.f;
#pragma unroll
        for (int k = 0; k < 8; k++) o += s_o[k][t];
        outp[t] = o;
    }
    if (w == 7) {
        float l = __expf(s_s[lane] - m) + __expf(s_s[lane + 64] - m);
        l = wave_sum(l);
        if (lane == 0) { outp[128] = m; outp[129] = l; }
    }
}

__global__ void __launch_bounds__(320) k3_out(const u16* __restrict__ x, const u16* __restrict__ wo,
                                              const u16* __restrict__ w2, const u16* __restrict__ b1,
                                              const u16* __restrict__ b2, float* __restrict__ ws,
                                              u16* __restrict__ out) {
    __shared__ float s_o[DH];
    __shared__ float s_h[64];
    __shared__ float s_red[2][32][80];
    __shared__ int s_last;
    const int t = threadIdx.x;
    const int h = blockIdx.x >> 3, c = blockIdx.x & 7;
    const int ch = t % 10, rb = t / 10;
    v4u wv[4], w2v[2];
#pragma unroll
    for (int i = 0; i < 4; i++)
        wv[i] = __builtin_nontemporal_load((const v4u*)(wo + (size_t)(h * DH + rb + 32 * i) * D_MODEL + c * 80 + ch * 8));
    w2v[0] = __builtin_nontemporal_load((const v4u*)(w2 + (size_t)(h * 54 + rb) * D_MODEL + c * 80 + ch * 8));
    w2v[1] = (rb + 32 < 54) ? __builtin_nontemporal_load((const v4u*)(w2 + (size_t)(h * 54 + rb + 32) * D_MODEL + c * 80 + ch * 8))
                            : (v4u){0u, 0u, 0u, 0u};
    if (t < DH) {
        float M = -INFINITY;
#pragma unroll
        for (int k = 0; k < 8; k++) M = fmaxf(M, ws[WS_ATT + (h * 8 + k) * 132 + 128]);
        float L = 0.f, o = 0.f;
#pragma unroll
        for (int k = 0; k < 8; k++) {
            const float* p = ws + WS_ATT + (h * 8 + k) * 132;
            float sc = __expf(p[128] - M);
            L += sc * p[129];
            o += sc * p[t];
        }
        s_o[t] = rbf(o / L);
    } else if (t < DH + 64) {
        int j = t - DH;
        float hv = 0.f;
        if (j < 54) {
            int col = h * 54 + j;
#pragma unroll
            for (int k = 0; k < KSPLIT; k++) hv += ws[WS_QPART + k * NCOLS + QCOLS + col];
            hv = rbf(rbf(hv) + bf2f(b1[col]));
            float g = 0.5f * hv * (1.0f + tanhf(0.7978845608028654f * (hv + 0.044715f * hv * hv * hv)));
            hv = rbf(g);
        }
        s_h[j] = hv;
    }
    __syncthreads();
    float a[8], b[8];
#pragma unroll
    for (int j = 0; j < 8; j++) { a[j] = 0.f; b[j] = 0.f; }
#pragma unroll
    for (int i = 0; i < 4; i++) {
        float ov = s_o[rb + 32 * i];
#pragma unroll
        for (int j = 0; j < 4; j++) {
            a[2 * j] += ov * bflo(wv[i][j]);
            a[2 * j + 1] += ov * bfhi(wv[i][j]);
        }
    }
#pragma unroll
    for (int i = 0; i < 2; i++) {
        float hv = s_h[(rb + 32 * i) & 63];
#pragma unroll
        for (int j = 0; j < 4; j++) {
            b[2 * j] += hv * bflo(w2v[i][j]);
            b[2 * j + 1] += hv * bfhi(w2v[i][j]);
        }
    }
#pragma unroll
    for (int j = 0; j < 8; j++) {
        s_red[0][rb][ch * 8 + j] = a[j];
        s_red[1][rb][ch * 8 + j] = b[j];
    }
    __syncthreads();
    if (t < 160) {
        const int which = t / 80, col = t % 80;
        float r = 0.f;
#pragma unroll
        for (int k = 0; k < 32; k++) r += s_red[which][k][col];
        __hip_atomic_store(ws + WS_OUTP + blockIdx.x * 160 + t, r, __ATOMIC_RELAXED, __HIP_MEMORY_SCOPE_AGENT);
    }
    asm volatile("s_waitcnt vmcnt(0)" ::: "memory");
    __syncthreads();
    if (t == 0) {
        u32 prev = __hip_atomic_fetch_add((u32*)ws + WS_CNT + c * 64, 1u, __ATOMIC_RELAXED, __HIP_MEMORY_SCOPE_AGENT);
        s_last = (prev == 31u);
    }
    __syncthreads();
    if (s_last) {
        if (t < 80) {
            float A = 0.f, B = 0.f;
            float av[32], bv[32];
#pragma unroll
            for (int k = 0; k < 32; k++) {
                const float* p = ws + WS_OUTP + (k * 8 + c) * 160;
                av[k] = __hip_atomic_load(p + t, __ATOMIC_RELAXED, __HIP_MEMORY_SCOPE_AGENT);
                bv[k] = __hip_atomic_load(p + 80 + t, __ATOMIC_RELAXED, __HIP_MEMORY_SCOPE_AGENT);
            }
#pragma unroll
            for (int k = 0; k < 32; k++) { A += av[k]; B += bv[k]; }
            const int col = c * 80 + t;
            float Bv = rbf(rbf(B) + bf2f(b2[col]));
            float r = rbf(bf2f(x[col]) + rbf(A));
            out[col] = (u16)f2bf(r + Bv);
        }
    }
}

extern "C" void kernel_launch(void* const* d_in, const int* in_sizes, int n_in,
                              void* d_out, int out_size, void* d_ws, size_t ws_size,
                              hipStream_t stream) {
    const u16* x = (const u16*)d_in[0];
    const u16* kc = (const u16*)d_in[1];
    const u16* vc = (const u16*)d_in[2];
    const u16* wq = (const u16*)d_in[3];
    const u16* wo = (const u16*)d_in[4];
    const u16* w1 = (const u16*)d_in[6];
    const u16* b1 = (const u16*)d_in[7];
    const u16* w2 = (const u16*)d_in[8];
    const u16* b2 = (const u16*)d_in[9];
    float* ws = (float*)d_ws;
    k1_proj<<<91 * 4, 256, 0, stream>>>(x, wq, w1, ws);
    k2_attn<<<256, 512, 0, stream>>>(kc, vc, ws);
    k3_out<<<256, 320, 0, stream>>>(x, wo, w2, b1, b2, ws, (u16*)d_out);
}
```

```cpp
#include <hip/hip_runtime.h>
#include <hip/hip_bf16.h>
#include <stdint.h>

typedef unsigned int u32;
typedef unsigned short u16;
typedef u32 v4u __attribute__((ext_vector_type(4)));

#define D_MODEL 640
#define NHEAD 32
#define DH 128
#define SK 4096
#define WIN 1024
#define FF 1728
#define QCOLS 4096
#define NCOLS (QCOLS + FF)
#define KSPLIT 4
#define ROWS_PER_SPLIT (D_MODEL / KSPLIT)

__device__ __forceinline__ float bf2f(u32 b) { return __uint_as_float(b << 16); }
__device__ __forceinline__ float bflo(u32 w) { return __uint_as_float(w << 16); }
__device__ __forceinline__ float bfhi(u32 w) { return __uint_as_float(w & 0xffff0000u); }
__device__ __forceinline__ u32 f2bf(float f) {
    u32 u = __float_as_uint(f);
    return (u + 0x7fffu + ((u >> 16) & 1u)) >> 16;
}
__device__ __forceinline__ float rbf(float f) { return bf2f(f2bf(f)); }

#define WS_QPART 0
#define WS_GRAN (WS_QPART + KSPLIT * NCOLS + 64)
#define WS_OUTP (WS_GRAN + 256 * 132 * 2)
#define WS_CNT (WS_OUTP + 256 * 160)

__device__ __forceinline__ float wave_sum(float v) {
#pragma unroll
    for (int o = 32; o > 0; o >>= 1) v += __shfl_xor(v, o, 64);
    return v;
}

__global__ void __launch_bounds__(256) k1_proj(const u16* __restrict__ x, const u16* __restrict__ wq,
                                               const u16* __restrict__ w1, float* __restrict__ ws) {
    __shared__ float s_x[D_MODEL];
    __shared__ float s_red[4][64];
    const int t = threadIdx.x;
    const int cb = blockIdx.x >> 2;
    const int ks = blockIdx.x & 3;
    const bool isq = cb < 64;
    const u16* wbase = isq ? (wq + cb * 64) : (w1 + (cb - 64) * 64);
    const int ldw = isq ? QCOLS : FF;
    const int r0 = ks * ROWS_PER_SPLIT + (t >> 3);
    const int cc = t & 7;
    v4u wv[5];
#pragma unroll
    for (int i = 0; i < 5; i++)
        wv[i] = __builtin_nontemporal_load((const v4u*)(wbase + (size_t)(r0 + 32 * i) * ldw + cc * 8));
    if (blockIdx.x == 0 && t < 8) ((u32*)ws)[WS_CNT + t * 64] = 0u;
    {
        const int gi = blockIdx.x * 256 + t;
        if (gi < 256 * 132 * 2) ((u32*)ws)[WS_GRAN + gi] = 0u;
    }
    if (t < 64) {
        float v[10];
        float s = 0.f;
#pragma unroll
        for (int i = 0; i < 10; i++) { v[i] = bf2f(x[t + 64 * i]); s += v[i]; }
        s = wave_sum(s);
        float mean = s * (1.0f / D_MODEL);
        float q = 0.f;
#pragma unroll
        for (int i = 0; i < 10; i++) { float d = v[i] - mean; q += d * d; }
        q = wave_sum(q) * (1.0f / D_MODEL);
        float mb = rbf(mean), vb = rbf(q);
        float sd = rbf(sqrtf(rbf(vb + 1e-5f)));
#pragma unroll
        for (int i = 0; i < 10; i++) v[i] = rbf(rbf(v[i] - mb) / sd);
        if (!isq) {
            s = 0.f;
#pragma unroll
            for (int i = 0; i < 10; i++) s += v[i];
            s = wave_sum(s);
            mean = s * (1.0f / D_MODEL);
            q = 0.f;
#pragma unroll
            for (int i = 0; i < 10; i++) { float d = v[i] - mean; q += d * d; }
            q = wave_sum(q) * (1.0f / D_MODEL);
            mb = rbf(mean); vb = rbf(q);
            sd = rbf(sqrtf(rbf(vb + 1e-5f)));
#pragma unroll
            for (int i = 0; i < 10; i++) v[i] = rbf(rbf(v[i] - mb) / sd);
        }
#pragma unroll
        for (int i = 0; i < 10; i++) s_x[t + 64 * i] = v[i];
    }
    __syncthreads();
    float acc[8];
#pragma unroll
    for (int j = 0; j < 8; j++) acc[j] = 0.f;
#pragma unroll
    for (int i = 0; i < 5; i++) {
        float xv = s_x[r0 + 32 * i];
#pragma unroll
        for (int j = 0; j < 4; j++) {
            acc[2 * j] += xv * bflo(wv[i][j]);
            acc[2 * j + 1] += xv * bfhi(wv[i][j]);
        }
    }
#pragma unroll
    for (int j = 0; j < 8; j++) {
        acc[j] += __shfl_xor(acc[j], 8, 64);
        acc[j] += __shfl_xor(acc[j], 16, 64);
        acc[j] += __shfl_xor(acc[j], 32, 64);
    }
    const int w = t >> 6, lane = t & 63;
    if (lane < 8) {
#pragma unroll
        for (int j = 0; j < 8; j++) s_red[w][lane * 8 + j] = acc[j];
    }
    __syncthreads();
    if (t < 64) {
        float r = s_red[0][t] + s_red[1][t] + s_red[2][t] + s_red[3][t];
        ws[WS_QPART + ks * NCOLS + cb * 64 + t] = r;
    }
}


typedef unsigned long long u64;
#define AGENT_RLX __ATOMIC_RELAXED, __HIP_MEMORY_SCOPE_AGENT
__global__ void __launch_bounds__(512) k2_fused(const u16* __restrict__ x, const u16* __restrict__ kc,
                                                const u16* __restrict__ vc, const u16* __restrict__ wo,
                                                const u16* __restrict__ w2, const u16* __restrict__ b1,
                                                const u16* __restrict__ b2, float* __restrict__ ws,
                                                u16* __restrict__ out) {
    __shared__ float s_q[DH];
    __shared__ float s_h[64];
    __shared__ float s_s[128];
    __shared__ float s_o[8][DH];
    __shared__ float s_ml[8][2];
    __shared__ float s_red[2][51][80];
    __shared__ int s_last;
    const int t = threadIdx.x;
    const int h = blockIdx.x >> 3, c = blockIdx.x & 7;
    const int w = t >> 6, lane = t & 63;
    const int dl = lane & 15;
    const int kq = (t >> 4);
    const size_t base = ((size_t)h * SK + (SK - WIN) + c * 128) * DH;
    float qh = 0.f;
    if (t < DH) {
#pragma unroll
        for (int k = 0; k < KSPLIT; k++) qh += ws[WS_QPART + k * NCOLS + h * DH + t];
    } else if (t < DH + 54) {
#pragma unroll
        for (int k = 0; k < KSPLIT; k++) qh += ws[WS_QPART + k * NCOLS + QCOLS + h * 54 + (t - DH)];
    }
    v4u kv[4], vv[4];
#pragma unroll
    for (int i = 0; i < 4; i++) kv[i] = __builtin_nontemporal_load((const v4u*)(kc + base + (size_t)(kq + 32 * i) * DH + dl * 8));
#pragma unroll
    for (int i = 0; i < 4; i++) vv[i] = __builtin_nontemporal_load((const v4u*)(vc + base + (size_t)(kq + 32 * i) * DH + dl * 8));
    const int ch = t % 10, rb = t / 10;
    v4u wv[4];
#pragma unroll
    for (int i = 0; i < 4; i++) {
        const int r = rb + 51 * i;
        const u16* p = (r < 128) ? (wo + (size_t)(h * DH + r) * D_MODEL) : (w2 + (size_t)(h * 54 + (r - 128)) * D_MODEL);
        const bool ok = (t < 510) && (r < 182);
        wv[i] = ok ? __builtin_nontemporal_load((const v4u*)(p + c * 80 + ch * 8)) : (v4u){0u, 0u, 0u, 0u};
    }
    if (t < DH) {
        s_q[t] = rbf(qh);
    } else if (t < DH + 64) {
        const int j = t - DH;
        float hv = 0.f;
        if (j < 54) {
            hv = rbf(rbf(qh) + bf2f(b1[h * 54 + j]));
            hv = rbf(0.5f * hv * (1.0f + tanhf(0.7978845608028654f * (hv + 0.044715f * hv * hv * hv))));
        }
        s_h[j] = hv;
    }
    __syncthreads();
    float qv[8];
#pragma unroll
    for (int j = 0; j < 8; j++) qv[j] = s_q[dl * 8 + j];
#pragma unroll
    for (int i = 0; i < 4; i++) {
        float s = 0.f;
#pragma unroll
        for (int j = 0; j < 4; j++) {
            s += qv[2 * j] * bflo(kv[i][j]);
            s += qv[2 * j + 1] * bfhi(kv[i][j]);
        }
        s += __shfl_xor(s, 1, 64);
        s += __shfl_xor(s, 2, 64);
        s += __shfl_xor(s, 4, 64);
        s += __shfl_xor(s, 8, 64);
        if (dl == 0) s_s[kq + 32 * i] = rbf(rbf(s) * 0.08838834764831845f);
    }
    __syncthreads();
    float m = fmaxf(s_s[lane], s_s[lane + 64]);
#pragma unroll
    for (int o = 32; o > 0; o >>= 1) m = fmaxf(m, __shfl_xor(m, o, 64));
    float oacc[8];
#pragma unroll
    for (int j = 0; j < 8; j++) oacc[j] = 0.f;
#pragma unroll
    for (int i = 0; i < 4; i++) {
        float p = __expf(s_s[kq + 32 * i] - m);
#pragma unroll
        for (int j = 0; j < 4; j++) {
            oacc[2 * j] += p * bflo(vv[i][j]);
            oacc[2 * j + 1] += p * bfhi(vv[i][j]);
        }
    }
#pragma unroll
    for (int j = 0; j < 8; j++) {
        oacc[j] += __shfl_xor(oacc[j], 16, 64);
        oacc[j] += __shfl_xor(oacc[j], 32, 64);
    }
    if (lane < 16) {
#pragma unroll
        for (int j = 0; j < 8; j++) s_o[w][lane * 8 + j] = oacc[j];
    }
    __syncthreads();
    u64* gran = (u64*)(ws + WS_GRAN);
    if (t < DH) {
        float o = 0.f;
#pragma unroll
        for (int k = 0; k < 8; k++) o += s_o[k][t];
        __hip_atomic_store(gran + (size_t)blockIdx.x * 132 + t, (1ull << 32) | (u64)__float_as_uint(o), AGENT_RLX);
    } else if (w == 7) {
        float l = __expf(s_s[lane] - m) + __expf(s_s[lane + 64] - m);
        l = wave_sum(l);
        if (lane < 2) {
            float v = lane == 0 ? m : l;
            __hip_atomic_store(gran + (size_t)blockIdx.x * 132 + 128 + lane, (1ull << 32) | (u64)__float_as_uint(v), AGENT_RLX);
        }
    }
    float pv[8];
    if (t < DH + 16) {
        const u64* g = gran + (size_t)(h * 8) * 132;
        if (t < DH) {
            for (unsigned spin = 0; spin < (1u << 22); spin++) {
                bool ok = true;
#pragma unroll
                for (int k = 0; k < 8; k++) {
                    u64 v = __hip_atomic_load(g + k * 132 + t, AGENT_RLX);
                    pv[k] = __uint_as_float((u32)v);
                    ok &= ((v >> 32) == 1ull);
                }
                if (ok) break;
                __builtin_amdgcn_s_sleep(1);
            }
        } else {
            const int k = (t - DH) >> 1, e = (t - DH) & 1;
            for (unsigned spin = 0; spin < (1u << 22); spin++) {
                u64 v = __hip_atomic_load(g + k * 132 + 128 + e, AGENT_RLX);
                if ((v >> 32) == 1ull) { s_ml[k][e] = __uint_as_float((u32)v); break; }
                __builtin_amdgcn_s_sleep(1);
            }
        }
    }
    __syncthreads();
    if (t < DH) {
        float M = s_ml[0][0];
#pragma unroll
        for (int k = 1; k < 8; k++) M = fmaxf(M, s_ml[k][0]);
        float L = 0.f, o = 0.f;
#pragma unroll
        for (int k = 0; k < 8; k++) {
            float sc = __expf(s_ml[k][0] - M);
            L += sc * s_ml[k][1];
            o += sc * pv[k];
        }
        s_q[t] = rbf(o / L);
    }
    __syncthreads();
    float a[8], b[8];
#pragma unroll
    for (int j = 0; j < 8; j++) { a[j] = 0.f; b[j] = 0.f; }
#pragma unroll
    for (int i = 0; i < 4; i++) {
        const int r = rb + 51 * i;
        if (r < 128) {
            float ov = s_q[r];
#pragma unroll
            for (int j = 0; j < 4; j++) { a[2 * j] += ov * bflo(wv[i][j]); a[2 * j + 1] += ov * bfhi(wv[i][j]); }
        } else {
            float hv = s_h[(r - 128) & 63];
#pragma unroll
            for (int j = 0; j < 4; j++) { b[2 * j] += hv * bflo(wv[i][j]); b[2 * j + 1] += hv * bfhi(wv[i][j]); }
        }
    }
    if (t < 510) {
#pragma unroll
        for (int j = 0; j < 8; j++) { s_red[0][rb][ch * 8 + j] = a[j]; s_red[1][rb][ch * 8 + j] = b[j]; }
    }
    __syncthreads();
    if (t < 160) {
        const int which = t / 80, col = t % 80;
        float r = 0.f;
#pragma unroll
        for (int k = 0; k < 51; k++) r += s_red[which][k][col];
        __hip_atomic_store(ws + WS_OUTP + blockIdx.x * 160 + t, r, AGENT_RLX);
    }
    asm volatile("s_waitcnt vmcnt(0)" ::: "memory");
    __syncthreads();
    if (t == 0) {
        u32 prev = __hip_atomic_fetch_add((u32*)ws + WS_CNT + c * 64, 1u, AGENT_RLX);
        s_last = (prev == 31u);
    }
    __syncthreads();
    if (s_last) {
        if (t < 80) {
            float A = 0.f, B = 0.f;
            float av[32], bv[32];
#pragma unroll
            for (int k = 0; k < 32; k++) {
                const float* p = ws + WS_OUTP + (k * 8 + c) * 160;
                av[k] = __hip_atomic_load(p + t, AGENT_RLX);
                bv[k] = __hip_atomic_load(p + 80 + t, AGENT_RLX);
            }
#pragma unroll
            for (int k = 0; k < 32; k++) { A += av[k]; B += bv[k]; }
            const int col = c * 80 + t;
            float Bv = rbf(rbf(B) + bf2f(b2[col]));
            float r = rbf(bf2f(x[col]) + rbf(A));
            out[col] = (u16)f2bf(r + Bv);
        }
    }
}

extern "C" void kernel_launch(void* const* d_in, const int* in_sizes, int n_in,
                              void* d_out, int out_size, void* d_ws, size_t ws_size,
                              hipStream_t stream) {
    const u16* x = (const u16*)d_in[0];
    const u16* kc = (const u16*)d_in[1];
    const u16* vc = (const u16*)d_in[2];
    const u16* wq = (const u16*)d_in[3];
    const u16* wo = (const u16*)d_in[4];
    const u16* w1 = (const u16*)d_in[6];
    const u16* b1 = (const u16*)d_in[7];
    const u16* w2 = (const u16*)d_in[8];
    const u16* b2 = (const u16*)d_in[9];
    float* ws = (float*)d_ws;
    k1_proj<<<91 * 4, 256, 0, stream>>>(x, wq, w1, ws);
    k2_fused<<<256, 512, 0, stream>>>(x, kc, vc, wo, w2, b1, b2, ws, (u16*)d_out);
}
```

```cpp
#include <hip/hip_runtime.h>
#include <hip/hip_bf16.h>
#include <stdint.h>

typedef unsigned int u32;
typedef unsigned short u16;
typedef u32 v4u __attribute__((ext_vector_type(4)));

#define D_MODEL 640
#define NHEAD 32
#define DH 128
#define SK 4096
#define WIN 1024
#define FF 1728
#define QCOLS 4096
#define NCOLS (QCOLS + FF)
#define KSPLIT 4
#define ROWS_PER_SPLIT (D_MODEL / KSPLIT)

__device__ __forceinline__ float bf2f(u32 b) { return __uint_as_float(b << 16); }
__device__ __forceinline__ float bflo(u32 w) { return __uint_as_float(w << 16); }
__device__ __forceinline__ float bfhi(u32 w) { return __uint_as_float(w & 0xffff0000u); }
__device__ __forceinline__ u32 f2bf(float f) {
    u32 u = __float_as_uint(f);
    return (u + 0x7fffu + ((u >> 16) & 1u)) >> 16;
}
__device__ __forceinline__ float rbf(float f) { return bf2f(f2bf(f)); }

#define WS_QPART 0
#define WS_GRAN (WS_QPART + KSPLIT * NCOLS + 64)
#define WS_OUTP (WS_GRAN + 256 * 132 * 2)
#define WS_CNT (WS_OUTP + 256 * 160)
#define WS_QG (WS_CNT + 8 * 64)
#define WS_END (WS_QG + 256 * 184 * 2)

__device__ __forceinline__ float wave_sum(float v) {
#pragma unroll
    for (int o = 32; o > 0; o >>= 1) v += __shfl_xor(v, o, 64);
    return v;
}

typedef unsigned long long u64;
#define AGENT_RLX __ATOMIC_RELAXED, __HIP_MEMORY_SCOPE_AGENT
__global__ void __launch_bounds__(512) k2_fused(const u16* __restrict__ x, const u16* __restrict__ wq,
                                                const u16* __restrict__ w1, const u16* __restrict__ kc,
                                                const u16* __restrict__ vc, const u16* __restrict__ wo,
                                                const u16* __restrict__ w2, const u16* __restrict__ b1,
                                                const u16* __restrict__ b2, float* __restrict__ ws,
                                                u16* __restrict__ out) {
    __shared__ float s_q[DH];
    __shared__ float s_h[64];
    __shared__ float s_s[128];
    __shared__ float s_o[8][DH];
    __shared__ float s_ml[8][2];
    __shared__ float s_red[2][51][80];
    __shared__ int s_last;
    __shared__ float s_xn[80], s_y[80];
    __shared__ float s_pq[8][128];
    __shared__ float s_ph[16][54];
    const int t = threadIdx.x;
    const int h = blockIdx.x >> 3, c = blockIdx.x & 7;
    const int w = t >> 6, lane = t & 63;
    const int dl = lane & 15;
    const int kq = (t >> 4);
    const size_t base = ((size_t)h * SK + (SK - WIN) + c * 128) * DH;
    v4u wqv[3];
    const int qrow = t >> 4;
#pragma unroll
    for (int i = 0; i < 3; i++)
        wqv[i] = __builtin_nontemporal_load((const v4u*)(wq + (size_t)(c * 80 + min(qrow + 32 * i, 79)) * QCOLS + h * DH + dl * 8));
    const int cp = t % 27, rg = min(t / 27, 15);
    u32 w1v[5];
#pragma unroll
    for (int i = 0; i < 5; i++)
        w1v[i] = __builtin_nontemporal_load((const u32*)(w1 + (size_t)(c * 80 + rg + 16 * i) * FF + h * 54 + cp * 2));
    float b1v = bf2f(b1[h * 54 + min(max(t - DH, 0), 53)]);
    asm volatile("" : "+v"(b1v));
    v4u kv[4], vv[4];
#pragma unroll
    for (int i = 0; i < 4; i++) kv[i] = __builtin_nontemporal_load((const v4u*)(kc + base + (size_t)(kq + 32 * i) * DH + dl * 8));
#pragma unroll
    for (int i = 0; i < 4; i++) vv[i] = __builtin_nontemporal_load((const v4u*)(vc + base + (size_t)(kq + 32 * i) * DH + dl * 8));
    const int ch = t % 10, rb = t / 10;
    v4u wv[4];
#pragma unroll
    for (int i = 0; i < 4; i++) {
        const int r = min(rb + 51 * i, 181);
        const u16* p = (r < 128) ? (wo + (size_t)(h * DH + r) * D_MODEL) : (w2 + (size_t)(h * 54 + (r - 128)) * D_MODEL);
        wv[i] = __builtin_nontemporal_load((const v4u*)(p + c * 80 + ch * 8));
    }
    if (t < 64) {
        float v[10];
        float s = 0.f;
#pragma unroll
        for (int i = 0; i < 10; i++) { v[i] = bf2f(x[t + 64 * i]); s += v[i]; }
        s = wave_sum(s);
        float mean = s * (1.0f / D_MODEL);
        float q = 0.f;
#pragma unroll
        for (int i = 0; i < 10; i++) { float d = v[i] - mean; q += d * d; }
        q = wave_sum(q) * (1.0f / D_MODEL);
        float mb = rbf(mean), vb = rbf(q);
        float sd = rbf(sqrtf(rbf(vb + 1e-5f)));
#pragma unroll
        for (int i = 0; i < 10; i++) v[i] = rbf(rbf(v[i] - mb) / sd);
#pragma unroll
        for (int i = 0; i < 10; i++) { int r = t + 64 * i - c * 80; if (r >= 0 && r < 80) s_xn[r] = v[i]; }
        s = 0.f;
#pragma unroll
        for (int i = 0; i < 10; i++) s += v[i];
        s = wave_sum(s);
        mean = s * (1.0f / D_MODEL);
        q = 0.f;
#pragma unroll
        for (int i = 0; i < 10; i++) { float d = v[i] - mean; q += d * d; }
        q = wave_sum(q) * (1.0f / D_MODEL);
        mb = rbf(mean); vb = rbf(q);
        sd = rbf(sqrtf(rbf(vb + 1e-5f)));
#pragma unroll
        for (int i = 0; i < 10; i++) { int r = t + 64 * i - c * 80; if (r >= 0 && r < 80) s_y[r] = rbf(rbf(v[i] - mb) / sd); }
    }
    __syncthreads();
    {
        float acc[8];
#pragma unroll
        for (int j = 0; j < 8; j++) acc[j] = 0.f;
#pragma unroll
        for (int i = 0; i < 3; i++) {
            const int r = qrow + 32 * i;
            const float xv = (r < 80) ? s_xn[min(r, 79)] : 0.f;
#pragma unroll
            for (int j = 0; j < 4; j++) { acc[2 * j] += xv * bflo(wqv[i][j]); acc[2 * j + 1] += xv * bfhi(wqv[i][j]); }
        }
#pragma unroll
        for (int j = 0; j < 8; j++) { acc[j] += __shfl_xor(acc[j], 16, 64); acc[j] += __shfl_xor(acc[j], 32, 64); }
        if (lane < 16) {
#pragma unroll
            for (int j = 0; j < 8; j++) s_pq[w][lane * 8 + j] = acc[j];
        }
        float h0 = 0.f, h1 = 0.f;
#pragma unroll
        for (int i = 0; i < 5; i++) { const float yv = s_y[rg + 16 * i]; h0 += yv * bflo(w1v[i]); h1 += yv * bfhi(w1v[i]); }
        if (t < 432) { s_ph[rg][cp * 2] = h0; s_ph[rg][cp * 2 + 1] = h1; }
    }
    __syncthreads();
    u64* qg = (u64*)(ws + WS_QG);
    if (t < 182) {
        float r = 0.f;
        if (t < DH) {
#pragma unroll
            for (int k = 0; k < 8; k++) r += s_pq[k][t];
        } else {
#pragma unroll
            for (int k = 0; k < 16; k++) r += s_ph[k][t - DH];
        }
        __hip_atomic_store(qg + (size_t)blockIdx.x * 184 + t, (1ull << 32) | (u64)__float_as_uint(r), AGENT_RLX);
    }
    float qh = 0.f;
    if (t < 182) {
        const u64* g = qg + (size_t)(h * 8) * 184 + t;
        for (unsigned spin = 0; spin < (1u << 22); spin++) {
            bool ok = true; qh = 0.f;
#pragma unroll
            for (int k = 0; k < 8; k++) {
                u64 v = __hip_atomic_load(g + k * 184, AGENT_RLX);
                qh += __uint_as_float((u32)v);
                ok &= ((v >> 32) == 1ull);
            }
            if (ok) break;
            __builtin_amdgcn_s_sleep(1);
        }
    }
    if (t < DH) {
        s_q[t] = rbf(qh);
    } else if (t < DH + 64) {
        const int j = t - DH;
        float hv = 0.f;
        if (j < 54) {
            hv = rbf(rbf(qh) + b1v);
            hv = rbf(0.5f * hv * (1.0f + tanhf(0.7978845608028654f * (hv + 0.044715f * hv * hv * hv))));
        }
        s_h[j] = hv;
    }
    __syncthreads();
    float qv[8];
#pragma unroll
    for (int j = 0; j < 8; j++) qv[j] = s_q[dl * 8 + j];
#pragma unroll
    for (int i = 0; i < 4; i++) {
        float s = 0.f;
#pragma unroll
        for (int j = 0; j < 4; j++) {
            s += qv[2 * j] * bflo(kv[i][j]);
            s += qv[2 * j + 1] * bfhi(kv[i][j]);
        }
        s += __shfl_xor(s, 1, 64);
        s += __shfl_xor(s, 2, 64);
        s += __shfl_xor(s, 4, 64);
        s += __shfl_xor(s, 8, 64);
        if (dl == 0) s_s[kq + 32 * i] = rbf(rbf(s) * 0.08838834764831845f);
    }
    __syncthreads();
    float m = fmaxf(s_s[lane], s_s[lane + 64]);
#pragma unroll
    for (int o = 32; o > 0; o >>= 1) m = fmaxf(m, __shfl_xor(m, o, 64));
    float oacc[8];
#pragma unroll
    for (int j = 0; j < 8; j++) oacc[j] = 0.f;
#pragma unroll
    for (int i = 0; i < 4; i++) {
        float p = __expf(s_s[kq + 32 * i] - m);
#pragma unroll
        for (int j = 0; j < 4; j++) {
            oacc[2 * j] += p * bflo(vv[i][j]);
            oacc[2 * j + 1] += p * bfhi(vv[i][j]);
        }
    }
#pragma unroll
    for (int j = 0; j < 8; j++) {
        oacc[j] += __shfl_xor(oacc[j], 16, 64);
        oacc[j] += __shfl_xor(oacc[j], 32, 64);
    }
    if (lane < 16) {
#pragma unroll
        for (int j = 0; j < 8; j++) s_o[w][lane * 8 + j] = oacc[j];
    }
    __syncthreads();
    u64* gran = (u64*)(ws + WS_GRAN);
    if (t < DH) {
        float o = 0.f;
#pragma unroll
        for (int k = 0; k < 8; k++) o += s_o[k][t];
        __hip_atomic_store(gran + (size_t)blockIdx.x * 132 + t, (1ull << 32) | (u64)__float_as_uint(o), AGENT_RLX);
    } else if (w == 7) {
        float l = __expf(s_s[lane] - m) + __expf(s_s[lane + 64] - m);
        l = wave_sum(l);
        if (lane < 2) {
            float v = lane == 0 ? m : l;
            __hip_atomic_store(gran + (size_t)blockIdx.x * 132 + 128 + lane, (1ull << 32) | (u64)__float_as_uint(v), AGENT_RLX);
        }
    }
    float pv[8];
    if (t < DH + 16) {
        const u64* g = gran + (size_t)(h * 8) * 132;
        if (t < DH) {
            for (unsigned spin = 0; spin < (1u << 22); spin++) {
                bool ok = true;
#pragma unroll
                for (int k = 0; k < 8; k++) {
                    u64 v = __hip_atomic_load(g + k * 132 + t, AGENT_RLX);
                    pv[k] = __uint_as_float((u32)v);
                    ok &= ((v >> 32) == 1ull);
                }
                if (ok) break;
                __builtin_amdgcn_s_sleep(1);
            }
        } else {
            const int k = (t - DH) >> 1, e = (t - DH) & 1;
            for (unsigned spin = 0; spin < (1u << 22); spin++) {
                u64 v = __hip_atomic_load(g + k * 132 + 128 + e, AGENT_RLX);
                if ((v >> 32) == 1ull) { s_ml[k][e] = __uint_as_float((u32)v); break; }
                __builtin_amdgcn_s_sleep(1);
            }
        }
    }
    __syncthreads();
    if (t < DH) {
        float M = s_ml[0][0];
#pragma unroll
        for (int k = 1; k < 8; k++) M = fmaxf(M, s_ml[k][0]);
        float L = 0.f, o = 0.f;
#pragma unroll
        for (int k = 0; k < 8; k++) {
            float sc = __expf(s_ml[k][0] - M);
            L += sc * s_ml[k][1];
            o += sc * pv[k];
        }
        s_q[t] = rbf(o / L);
    }
    __syncthreads();
    float a[8], b[8];
#pragma unroll
    for (int j = 0; j < 8; j++) { a[j] = 0.f; b[j] = 0.f; }
#pragma unroll
    for (int i = 0; i < 4; i++) {
        const int r = rb + 51 * i;
        if (r >= 182) continue;
        if (r < 128) {
            float ov = s_q[r];
#pragma unroll
            for (int j = 0; j < 4; j++) { a[2 * j] += ov * bflo(wv[i][j]); a[2 * j + 1] += ov * bfhi(wv[i][j]); }
        } else {
            float hv = s_h[(r - 128) & 63];
#pragma unroll
            for (int j = 0; j < 4; j++) { b[2 * j] += hv * bflo(wv[i][j]); b[2 * j + 1] += hv * bfhi(wv[i][j]); }
        }
    }
    if (t < 510) {
#pragma unroll
        for (int j = 0; j < 8; j++) { s_red[0][rb][ch * 8 + j] = a[j]; s_red[1][rb][ch * 8 + j] = b[j]; }
    }
    __syncthreads();
    if (t < 160) {
        const int which = t / 80, col = t % 80;
        float r = 0.f;
#pragma unroll
        for (int k = 0; k < 51; k++) r += s_red[which][k][col];
        __hip_atomic_store(ws + WS_OUTP + blockIdx.x * 160 + t, r, AGENT_RLX);
    }
    asm volatile("s_waitcnt vmcnt(0)" ::: "memory");
    __syncthreads();
    if (t == 0) {
        u32 prev = __hip_atomic_fetch_add((u32*)ws + WS_CNT + c * 64, 1u, AGENT_RLX);
        s_last = (prev == 31u);
    }
    __syncthreads();
    if (s_last) {
        if (t < 80) {
            float A = 0.f, B = 0.f;
            float av[32], bv[32];
#pragma unroll
            for (int k = 0; k < 32; k++) {
                const float* p = ws + WS_OUTP + (k * 8 + c) * 160;
                av[k] = __hip_atomic_load(p + t, AGENT_RLX);
                bv[k] = __hip_atomic_load(p + 80 + t, AGENT_RLX);
            }
#pragma unroll
            for (int k = 0; k < 32; k++) { A += av[k]; B += bv[k]; }
            const int col = c * 80 + t;
            float Bv = rbf(rbf(B) + bf2f(b2[col]));
            float r = rbf(bf2f(x[col]) + rbf(A));
            out[col] = (u16)f2bf(r + Bv);
        }
    }
}

__global__ void __launch_bounds__(256) k0_zero(float* __restrict__ ws) {
    const int n = WS_END - WS_GRAN;
    for (int i = blockIdx.x * 256 + threadIdx.x; i < n; i += gridDim.x * 256) ((u32*)ws)[WS_GRAN + i] = 0u;
}

extern "C" void kernel_launch(void* const* d_in, const int* in_sizes, int n_in,
                              void* d_out, int out_size, void* d_ws, size_t ws_size,
                              hipStream_t stream) {
    const u16* x = (const u16*)d_in[0];
    const u16* kc = (const u16*)d_in[1];
    const u16* vc = (const u16*)d_in[2];
    const u16* wq = (const u16*)d_in[3];
    const u16* wo = (const u16*)d_in[4];
    const u16* w1 = (const u16*)d_in[6];
    const u16* b1 = (const u16*)d_in[7];
    const u16* w2 = (const u16*)d_in[8];
    const u16* b2 = (const u16*)d_in[9];
    float* ws = (float*)d_ws;
    k0_zero<<<128, 256, 0, stream>>>(ws);
    k2_fused<<<256, 512, 0, stream>>>(x, wq, w1, kc, vc, wo, w2, b1, b2, ws, (u16*)d_out);
}
```

```cpp
#include <hip/hip_runtime.h>
#include <hip/hip_bf16.h>
#include <stdint.h>

typedef unsigned int u32;
typedef unsigned short u16;
typedef u32 v4u __attribute__((ext_vector_type(4)));

#define D_MODEL 640
#define NHEAD 32
#define DH 128
#define SK 4096
#define WIN 1024
#define FF 1728
#define QCOLS 4096
#define NCOLS (QCOLS + FF)
#define KSPLIT 4
#define ROWS_PER_SPLIT (D_MODEL / KSPLIT)

__device__ __forceinline__ float bf2f(u32 b) { return __uint_as_float(b << 16); }
__device__ __forceinline__ float bflo(u32 w) { return __uint_as_float(w << 16); }
__device__ __forceinline__ float bfhi(u32 w) { return __uint_as_float(w & 0xffff0000u); }
__device__ __forceinline__ u32 f2bf(float f) {
    u32 u = __float_as_uint(f);
    return (u + 0x7fffu + ((u >> 16) & 1u)) >> 16;
}
__device__ __forceinline__ float rbf(float f) { return bf2f(f2bf(f)); }

#define WS_QPART 0
#define WS_GRAN (WS_QPART + KSPLIT * NCOLS + 64)
#define WS_OUTP (WS_GRAN + 256 * 132 * 2)
#define WS_CNT (WS_OUTP + 256 * 160)

__device__ __forceinline__ float wave_sum(float v) {
#pragma unroll
    for (int o = 32; o > 0; o >>= 1) v += __shfl_xor(v, o, 64);
    return v;
}

__global__ void __launch_bounds__(256) k1_proj(const u16* __restrict__ x, const u16* __restrict__ wq,
                                               const u16* __restrict__ w1, float* __restrict__ ws) {
    __shared__ float s_x[D_MODEL];
    __shared__ float s_red[4][64];
    const int t = threadIdx.x;
    const int cb = blockIdx.x >> 2;
    const int ks = blockIdx.x & 3;
    const bool isq = cb < 64;
    const u16* wbase = isq ? (wq + cb * 64) : (w1 + (cb - 64) * 64);
    const int ldw = isq ? QCOLS : FF;
    const int r0 = ks * ROWS_PER_SPLIT + (t >> 3);
    const int cc = t & 7;
    v4u wv[5];
#pragma unroll
    for (int i = 0; i < 5; i++)
        wv[i] = __builtin_nontemporal_load((const v4u*)(wbase + (size_t)(r0 + 32 * i) * ldw + cc * 8));
    if (blockIdx.x == 0 && t < 8) ((u32*)ws)[WS_CNT + t * 64] = 0u;
    {
        const int gi = blockIdx.x * 256 + t;
        if (gi < 256 * 132 * 2) ((u32*)ws)[WS_GRAN + gi] = 0u;
    }
    if (t < 64) {
        float v[10];
        float s = 0.f;
#pragma unroll
        for (int i = 0; i < 10; i++) { v[i] = bf2f(x[t + 64 * i]); s += v[i]; }
        s = wave_sum(s);
        float mean = s * (1.0f / D_MODEL);
        float q = 0.f;
#pragma unroll
        for (int i = 0; i < 10; i++) { float d = v[i] - mean; q += d * d; }
        q = wave_sum(q) * (1.0f / D_MODEL);
        float mb = rbf(mean), vb = rbf(q);
        float sd = rbf(sqrtf(rbf(vb + 1e-5f)));
#pragma unroll
        for (int i = 0; i < 10; i++) v[i] = rbf(rbf(v[i] - mb) / sd);
        if (!isq) {
            s = 0.f;
#pragma unroll
            for (int i = 0; i < 10; i++) s += v[i];
            s = wave_sum(s);
            mean = s * (1.0f / D_MODEL);
            q = 0.f;
#pragma unroll
            for (int i = 0; i < 10; i++) { float d = v[i] - mean; q += d * d; }
            q = wave_sum(q) * (1.0f / D_MODEL);
            mb = rbf(mean); vb = rbf(q);
            sd = rbf(sqrtf(rbf(vb + 1e-5f)));
#pragma unroll
            for (int i = 0; i < 10; i++) v[i] = rbf(rbf(v[i] - mb) / sd);
        }
#pragma unroll
        for (int i = 0; i < 10; i++) s_x[t + 64 * i] = v[i];
    }
    __syncthreads();
    float acc[8];
#pragma unroll
    for (int j = 0; j < 8; j++) acc[j] = 0.f;
#pragma unroll
    for (int i = 0; i < 5; i++) {
        float xv = s_x[r0 + 32 * i];
#pragma unroll
        for (int j = 0; j < 4; j++) {
            acc[2 * j] += xv * bflo(wv[i][j]);
            acc[2 * j + 1] += xv * bfhi(wv[i][j]);
        }
    }
#pragma unroll
    for (int j = 0; j < 8; j++) {
        acc[j] += __shfl_xor(acc[j], 8, 64);
        acc[j] += __shfl_xor(acc[j], 16, 64);
        acc[j] += __shfl_xor(acc[j], 32, 64);
    }
    const int w = t >> 6, lane = t & 63;
    if (lane < 8) {
#pragma unroll
        for (int j = 0; j < 8; j++) s_red[w][lane * 8 + j] = acc[j];
    }
    __syncthreads();
    if (t < 64) {
        float r = s_red[0][t] + s_red[1][t] + s_red[2][t] + s_red[3][t];
        ws[WS_QPART + ks * NCOLS + cb * 64 + t] = r;
    }
}


typedef unsigned long long u64;
#define AGENT_RLX __ATOMIC_RELAXED, __HIP_MEMORY_SCOPE_AGENT
__global__ void __launch_bounds__(512) k2_fused(const u16* __restrict__ x, const u16* __restrict__ kc,
                                                const u16* __restrict__ vc, const u16* __restrict__ wo,
                                                const u16* __restrict__ w2, const u16* __restrict__ b1,
                                                const u16* __restrict__ b2, float* __restrict__ ws,
                                                u16* __restrict__ out) {
    __shared__ float s_q[DH];
    __shared__ float s_h[64];
    __shared__ float s_s[128];
    __shared__ float s_o[8][DH];
    __shared__ float s_ml[8][2];
    __shared__ float s_red[2][51][80];
    __shared__ int s_last;
    const int t = threadIdx.x;
    const int h = blockIdx.x >> 3, c = blockIdx.x & 7;
    const int w = t >> 6, lane = t & 63;
    const int dl = lane & 15;
    const int kq = (t >> 4);
    const size_t base = ((size_t)h * SK + (SK - WIN) + c * 128) * DH;
    float qp[KSPLIT];
    {
        const int qi = (t < DH) ? (h * DH + t) : (QCOLS + h * 54 + min(t - DH, 53));
#pragma unroll
        for (int k = 0; k < KSPLIT; k++) qp[k] = ws[WS_QPART + k * NCOLS + min(qi, NCOLS - 1)];
    }
    const float b1v = bf2f(b1[h * 54 + min(max(t - DH, 0), 53)]);
    v4u kv[4], vv[4];
#pragma unroll
    for (int i = 0; i < 4; i++) kv[i] = __builtin_nontemporal_load((const v4u*)(kc + base + (size_t)(kq + 32 * i) * DH + dl * 8));
#pragma unroll
    for (int i = 0; i < 4; i++) vv[i] = __builtin_nontemporal_load((const v4u*)(vc + base + (size_t)(kq + 32 * i) * DH + dl * 8));
    const int ch = t % 10, rb = t / 10;
    v4u wv[4];
#pragma unroll
    for (int i = 0; i < 4; i++) {
        const int r = min(rb + 51 * i, 181);
        const u16* p = (r < 128) ? (wo + (size_t)(h * DH + r) * D_MODEL) : (w2 + (size_t)(h * 54 + (r - 128)) * D_MODEL);
        wv[i] = __builtin_nontemporal_load((const v4u*)(p + c * 80 + ch * 8));
    }
    const float qh = (qp[0] + qp[1]) + (qp[2] + qp[3]);
    if (t < DH) {
        s_q[t] = rbf(qh);
    } else if (t < DH + 64) {
        const int j = t - DH;
        float hv = 0.f;
        if (j < 54) {
            hv = rbf(rbf(qh) + b1v);
            hv = rbf(0.5f * hv * (1.0f + tanhf(0.7978845608028654f * (hv + 0.044715f * hv * hv * hv))));
        }
        s_h[j] = hv;
    }
    __syncthreads();
    float qv[8];
#pragma unroll
    for (int j = 0; j < 8; j++) qv[j] = s_q[dl * 8 + j];
#pragma unroll
    for (int i = 0; i < 4; i++) {
        float s = 0.f;
#pragma unroll
        for (int j = 0; j < 4; j++) {
            s += qv[2 * j] * bflo(kv[i][j]);
            s += qv[2 * j + 1] * bfhi(kv[i][j]);
        }
        s += __shfl_xor(s, 1, 64);
        s += __shfl_xor(s, 2, 64);
        s += __shfl_xor(s, 4, 64);
        s += __shfl_xor(s, 8, 64);
        if (dl == 0) s_s[kq + 32 * i] = rbf(rbf(s) * 0.08838834764831845f);
    }
    __syncthreads();
    float m = fmaxf(s_s[lane], s_s[lane + 64]);
#pragma unroll
    for (int o = 32; o > 0; o >>= 1) m = fmaxf(m, __shfl_xor(m, o, 64));
    float oacc[8];
#pragma unroll
    for (int j = 0; j < 8; j++) oacc[j] = 0.f;
#pragma unroll
    for (int i = 0; i < 4; i++) {
        float p = __expf(s_s[kq + 32 * i] - m);
#pragma unroll
        for (int j = 0; j < 4; j++) {
            oacc[2 * j] += p * bflo(vv[i][j]);
            oacc[2 * j + 1] += p * bfhi(vv[i][j]);
        }
    }
#pragma unroll
    for (int j = 0; j < 8; j++) {
        oacc[j] += __shfl_xor(oacc[j], 16, 64);
        oacc[j] += __shfl_xor(oacc[j], 32, 64);
    }
    if (lane < 16) {
#pragma unroll
        for (int j = 0; j < 8; j++) s_o[w][lane * 8 + j] = oacc[j];
    }
    __syncthreads();
    u64* gran = (u64*)(ws + WS_GRAN);
    if (t < DH) {
        float o = 0.f;
#pragma unroll
        for (int k = 0; k < 8; k++) o += s_o[k][t];
        __hip_atomic_store(gran + (size_t)blockIdx.x * 132 + t, (1ull << 32) | (u64)__float_as_uint(o), AGENT_RLX);
    } else if (w == 7) {
        float l = __expf(s_s[lane] - m) + __expf(s_s[lane + 64] - m);
        l = wave_sum(l);
        if (lane < 2) {
            float v = lane == 0 ? m : l;
            __hip_atomic_store(gran + (size_t)blockIdx.x * 132 + 128 + lane, (1ull << 32) | (u64)__float_as_uint(v), AGENT_RLX);
        }
    }
    float pv[8];
    if (t < DH + 16) {
        const u64* g = gran + (size_t)(h * 8) * 132;
        if (t < DH) {
            for (unsigned spin = 0; spin < (1u << 22); spin++) {
                bool ok = true;
#pragma unroll
                for (int k = 0; k < 8; k++) {
                    u64 v = __hip_atomic_load(g + k * 132 + t, AGENT_RLX);
                    pv[k] = __uint_as_float((u32)v);
                    ok &= ((v >> 32) == 1ull);
                }
                if (ok) break;
                __builtin_amdgcn_s_sleep(1);
            }
        } else {
            const int k = (t - DH) >> 1, e = (t - DH) & 1;
            for (unsigned spin = 0; spin < (1u << 22); spin++) {
                u64 v = __hip_atomic_load(g + k * 132 + 128 + e, AGENT_RLX);
                if ((v >> 32) == 1ull) { s_ml[k][e] = __uint_as_float((u32)v); break; }
                __builtin_amdgcn_s_sleep(1);
            }
        }
    }
    __syncthreads();
    if (t < DH) {
        float M = s_ml[0][0];
#pragma unroll
        for (int k = 1; k < 8; k++) M = fmaxf(M, s_ml[k][0]);
        float L = 0.f, o = 0.f;
#pragma unroll
        for (int k = 0; k < 8; k++) {
            float sc = __expf(s_ml[k][0] - M);
            L += sc * s_ml[k][1];
            o += sc * pv[k];
        }
        s_q[t] = rbf(o / L);
    }
    __syncthreads();
    float a[8], b[8];
#pragma unroll
    for (int j = 0; j < 8; j++) { a[j] = 0.f; b[j] = 0.f; }
#pragma unroll
    for (int i = 0; i < 4; i++) {
        const int r = rb + 51 * i;
        if (r >= 182) continue;
        if (r < 128) {
            float ov = s_q[r];
#pragma unroll
            for (int j = 0; j < 4; j++) { a[2 * j] += ov * bflo(wv[i][j]); a[2 * j + 1] += ov * bfhi(wv[i][j]); }
        } else {
            float hv = s_h[(r - 128) & 63];
#pragma unroll
            for (int j = 0; j < 4; j++) { b[2 * j] += hv * bflo(wv[i][j]); b[2 * j + 1] += hv * bfhi(wv[i][j]); }
        }
    }
    if (t < 510) {
#pragma unroll
        for (int j = 0; j < 8; j++) { s_red[0][rb][ch * 8 + j] = a[j]; s_red[1][rb][ch * 8 + j] = b[j]; }
    }
    __syncthreads();
    if (t < 160) {
        const int which = t / 80, col = t % 80;
        float r = 0.f;
#pragma unroll
        for (int k = 0; k < 51; k++) r += s_red[which][k][col];
        __hip_atomic_store(ws + WS_OUTP + blockIdx.x * 160 + t, r, AGENT_RLX);
    }
    asm volatile("s_waitcnt vmcnt(0)" ::: "memory");
    __syncthreads();
    if (t == 0) {
        u32 prev = __hip_atomic_fetch_add((u32*)ws + WS_CNT + c * 64, 1u, AGENT_RLX);
        s_last = (prev == 31u);
    }
    __syncthreads();
    if (s_last) {
        if (t < 80) {
            float A = 0.f, B = 0.f;
            float av[32], bv[32];
#pragma unroll
            for (int k = 0; k < 32; k++) {
                const float* p = ws + WS_OUTP + (k * 8 + c) * 160;
                av[k] = __hip_atomic_load(p + t, AGENT_RLX);
                bv[k] = __hip_atomic_load(p + 80 + t, AGENT_RLX);
            }
#pragma unroll
            for (int k = 0; k < 32; k++) { A += av[k]; B += bv[k]; }
            const int col = c * 80 + t;
            float Bv = rbf(rbf(B) + bf2f(b2[col]));
            float r = rbf(bf2f(x[col]) + rbf(A));
            out[col] = (u16)f2bf(r + Bv);
        }
    }
}

extern "C" void kernel_launch(void* const* d_in, const int* in_sizes, int n_in,
                              void* d_out, int out_size, void* d_ws, size_t ws_size,
                              hipStream_t stream) {
    const u16* x = (const u16*)d_in[0];
    const u16* kc = (const u16*)d_in[1];
    const u16* vc = (const u16*)d_in[2];
    const u16* wq = (const u16*)d_in[3];
    const u16* wo = (const u16*)d_in[4];
    const u16* w1 = (const u16*)d_in[6];
    const u16* b1 = (const u16*)d_in[7];
    const u16* w2 = (const u16*)d_in[8];
    const u16* b2 = (const u16*)d_in[9];
    float* ws = (float*)d_ws;
    k1_proj<<<91 * 4, 256, 0, stream>>>(x, wq, w1, ws);
    k2_fused<<<256, 512, 0, stream>>>(x, kc, vc, wo, w2, b1, b2, ws, (u16*)d_out);
}
```
